```python
import torch

def _softmax(x, axis=-1):
    return torch.softmax(x, dim=axis)

def _gelu(x):
    return torch.nn.functional.gelu(x, approximate='tanh')

def _silu(x):
    return torch.nn.functional.silu(x)

def _sigmoid(x):
    return torch.sigmoid(x)

def _layernorm(x, axis=-1, eps=1e-05):
    m = x.mean(dim=axis, keepdim=True)
    v = x.var(dim=axis, keepdim=True, unbiased=False)
    return (x - m) / torch.sqrt(v + eps)

def _scatter_add(tgt, idx, src, axis=0):
    return tgt.clone().index_add_(axis, idx, src)

def _segment_sum(src, seg, n):
    out = torch.zeros((n,) + src.shape[1:], dtype=src.dtype)
    return out.index_add_(0, seg, src)

def _pad(x, pw):
    flat = tuple((v for pair in reversed(pw) for v in pair))
    return torch.nn.functional.pad(x, flat)
kernel_name = 'adaLN_cond__mha_block_cached__glu_gated_swiglu__decode'

def setup_inputs():

    _w = lambda a: (lambda t: t.to(torch.bfloat16) if t.is_floating_point() else t)(torch.as_tensor(a))

    def _A_setup():
        B, Sq, D = (2, 1, 512)
        H, Sk, Dh = (32, 8192, 128)
        return {'x': _w((lambda *s: torch.randn(*s, dtype=torch.bfloat16) * 0.1)(B, Sq, D)), 'k_cache': _w((lambda *s: torch.randn(*s, dtype=torch.bfloat16) * 0.8)(B, H, Sk, Dh)), 'v_cache': _w((lambda *s: torch.randn(*s, dtype=torch.bfloat16) * 3.2)(B, H, Sk, Dh)), 'wq': _w((lambda *s: torch.randn(*s, dtype=torch.bfloat16) * 0.8)(D, H * Dh) * D ** (-0.5)), 'wo': _w((lambda *s: torch.randn(*s, dtype=torch.bfloat16) * 3.2)(H * Dh, D) * (H * Dh) ** (-0.5))}
    _A_in = _A_setup()

    def _B_setup():
        B, S, D, F = (2, 1, 512, 1376)
        return {'x': _w((lambda *s: torch.randn(*s, dtype=torch.bfloat16) * 0.1)(B, S, D)), 'w1': _w((lambda *s: torch.randn(*s, dtype=torch.bfloat16) * 3.2)(D, F) * (2.0 / D) ** 0.5), 'w3': _w((lambda *s: torch.randn(*s, dtype=torch.bfloat16) * 3.2)(D, F) * (2.0 / D) ** 0.5), 'w2': _w((lambda *s: torch.randn(*s, dtype=torch.bfloat16) * 3.2)(F, D) * (2.0 / F) ** 0.5)}
    _B_in = _B_setup()
    return {'A_x': _A_in['x'], 'A_k_cache': _A_in['k_cache'], 'A_v_cache': _A_in['v_cache'], 'A_wq': _A_in['wq'], 'A_wo': _A_in['wo'], 'B_x': _B_in['x'], 'B_w1': _B_in['w1'], 'B_w3': _B_in['w3'], 'B_w2': _B_in['w2'], 'c': _w((lambda *s: torch.randn(*s, dtype=torch.bfloat16) * 0.2)(2, 512) * 8.0), 'w_mod': _w((lambda *s: torch.randn(*s, dtype=torch.bfloat16) * 0.2)(512, 6 * 512) * 512 ** (-0.5) * 8.0)}

def reference(A_x, A_k_cache, A_v_cache, A_wq, A_wo, B_x, B_w1, B_w3, B_w2, c, w_mod):

    def _A_ref(x, k_cache, v_cache, wq, wo):
        B, Sq, D = x.shape
        H, Dh = (32, 128)
        q = (lambda x, *d: x.permute(*d))((x @ wq).reshape(B, Sq, H, Dh), 0, 2, 1, 3)
        scores = torch.einsum('bhqd,bhkd->bhqk', q, k_cache) * Dh ** (-0.5)
        p = _softmax(scores, axis=-1)
        o = torch.einsum('bhqk,bhkd->bhqd', p, v_cache)
        return (lambda x, *d: x.permute(*d))(o, 0, 2, 1, 3).reshape(B, Sq, H * Dh) @ wo

    def _B_ref(x, w1, w3, w2):
        gate = torch.einsum('bsd,df->bsf', x, w1)
        up = torch.einsum('bsd,df->bsf', x, w3)
        h = _silu(gate) * up
        out = torch.einsum('bsf,fd->bsd', h, w2)
        return out
    x = A_x
    mod = (_silu(c) @ w_mod).reshape(c.shape[0], 6, 1, -1)
    sa, ca, ga, sb, cb, gb = (mod[:, 0].clone(), mod[:, 1].clone(), mod[:, 2].clone(), mod[:, 3].clone(), mod[:, 4].clone(), mod[:, 5].clone())
    h = _layernorm(x, axis=-1) * (1 + ca) + sa
    x = x + ga * _A_ref(h, A_k_cache, A_v_cache, A_wq, A_wo)
    h = _layernorm(x, axis=-1) * (1 + cb) + sb
    x = x + gb * _B_ref(h, B_w1, B_w3, B_w2)
    return x
```

```cpp
#include <hip/hip_runtime.h>
#include <hip/hip_bf16.h>
#include <stdint.h>

typedef unsigned short u16;
typedef unsigned v4u __attribute__((ext_vector_type(4)));
typedef __bf16 bf16x2 __attribute__((ext_vector_type(2)));

constexpr int D = 512, H = 32, SK = 8192, DH = 128, F = 1376, NB = 2;
constexpr int NSPLIT = 8;

__device__ __forceinline__ float bf2f(u16 b) { return __uint_as_float((unsigned)b << 16); }
__device__ __forceinline__ float rbf(float f) { return f; }
__device__ __forceinline__ float lo(unsigned u) { return __uint_as_float(u << 16); }
__device__ __forceinline__ float hi(unsigned u) { return __uint_as_float(u & 0xffff0000u); }
__device__ __forceinline__ float dot2(unsigned k, unsigned q, float acc) {
  return __builtin_amdgcn_fdot2_f32_bf16(__builtin_bit_cast(bf16x2, k), __builtin_bit_cast(bf16x2, q), acc, false);
}

__device__ void gemv_cols(const float* xs, int K, const u16* W, int N, int n0, float* red  , float* out0, float* out1) {
}

__global__ void __launch_bounds__(256) k_mod(const u16* c, const u16* wmod, float* mod) {
  __shared__ float xs[2][D];
  __shared__ float red[2][32][8 + 1];
  int tid = threadIdx.x;
  for (int i = tid; i < 2 * D; i += 256) { float v = bf2f(c[i]); xs[i / D][i % D] = rbf(v / (1.f + __expf(-v))); }
  __syncthreads();
  int cg = tid & 7, r0 = tid >> 3; int n0 = blockIdx.x * 64 + cg * 8;
  float a0[8] = {0}, a1[8] = {0};
  for (int r = r0; r < D; r += 32) {
    v4u w = *(const v4u*)(wmod + (size_t)r * 3072 + n0);
    float x0 = xs[0][r], x1 = xs[1][r];
    for (int j = 0; j < 4; ++j) { a0[2*j] += x0 * lo(w[j]); a0[2*j+1] += x0 * hi(w[j]); a1[2*j] += x1 * lo(w[j]); a1[2*j+1] += x1 * hi(w[j]); }
  }
  __shared__ float rr[2][32][64];
  for (int j = 0; j < 8; ++j) { rr[0][r0][cg*8+j] = a0[j]; rr[1][r0][cg*8+j] = a1[j]; }
  __syncthreads();
  if (tid < 128) { int b = tid >> 6, col = tid & 63; float s = 0; for (int r = 0; r < 32; ++r) s += rr[b][r][col]; mod[b * 3072 + blockIdx.x * 64 + col] = rbf(s); }
}

__device__ void ln_mod(const float* xrow  , const float* sh, const float* sc, float* hout, int tid, float* scratch) {
}

__device__ __forceinline__ void compute_h(const float* xin  , const float* mod, int offs, int offc, float (*hs)[D], int tid) {
  __shared__ float stat[2][2];
  int wave = tid >> 6, lane = tid & 63;
  if (wave < 2) {
    float v[8]; float s = 0;
    for (int j = 0; j < 8; ++j) { v[j] = xin[wave * D + lane + 64 * j]; s += v[j]; }
    for (int m = 32; m; m >>= 1) s += __shfl_xor(s, m);
    float mean = s / D; float q = 0;
    for (int j = 0; j < 8; ++j) { float d = v[j] - mean; q += d * d; }
    for (int m = 32; m; m >>= 1) q += __shfl_xor(q, m);
    float var = rbf(q / D); mean = rbf(mean);
    float den = rbf(sqrtf(rbf(var + 1e-5f)));
    for (int j = 0; j < 8; ++j) {
      int i = lane + 64 * j;
      float n = rbf(rbf(v[j] - mean) / den);
      float h = rbf(rbf(n * rbf(1.f + mod[wave * 3072 + offc + i])) + mod[wave * 3072 + offs + i]);
      hs[wave][i] = h;
    }
  }
  __syncthreads();
}

__global__ void __launch_bounds__(256) k_q(const u16* x, const float* mod, const u16* wq, u16* q) {
  __shared__ float xs[2][D]; __shared__ float hs[2][D];
  int tid = threadIdx.x;
  for (int i = tid; i < 2 * D; i += 256) xs[i / D][i % D] = bf2f(x[i]);
  __syncthreads();
  compute_h(&xs[0][0], mod, 0, 512, hs, tid);
  int cg = tid & 7, r0 = tid >> 3; int n0 = blockIdx.x * 64 + cg * 8;
  float a0[8] = {0}, a1[8] = {0};
  for (int r = r0; r < D; r += 32) {
    v4u w = *(const v4u*)(wq + (size_t)r * 4096 + n0);
    float x0 = hs[0][r], x1 = hs[1][r];
    for (int j = 0; j < 4; ++j) { a0[2*j] += x0 * lo(w[j]); a0[2*j+1] += x0 * hi(w[j]); a1[2*j] += x1 * lo(w[j]); a1[2*j+1] += x1 * hi(w[j]); }
  }
  __shared__ float rr[2][32][64];
  for (int j = 0; j < 8; ++j) { rr[0][r0][cg*8+j] = a0[j]; rr[1][r0][cg*8+j] = a1[j]; }
  __syncthreads();
  if (tid < 128) { int b = tid >> 6, col = tid & 63; float s = 0; for (int r = 0; r < 32; ++r) s += rr[b][r][col];
    __hip_bfloat16 hb = __float2bfloat16(s); q[b * 4096 + blockIdx.x * 64 + col] = *(u16*)&hb; }
}

template <int CTRL> __device__ __forceinline__ float dpp(float x) {
  return __builtin_bit_cast(float, __builtin_amdgcn_mov_dpp(__builtin_bit_cast(int, x), CTRL, 0xf, 0xf, true));
}
constexpr int XOR1 = 0xB1, XOR2 = 0x4E, XOR7 = 0x141, XOR8 = 0x128;

__global__ void __launch_bounds__(256) k_attn(const u16* q, const u16* K, const u16* V, float* part  ) {
  __shared__ float mrg[4][132];
  int s = blockIdx.x, bh = blockIdx.y, tid = threadIdx.x, lane = tid & 63, wave = tid >> 6, r = lane >> 4, c = lane & 15;
  constexpr int KEYS = SK / NSPLIT;
  v4u qv = *(const v4u*)(q + bh * DH + 8 * c);
  const u16* kb = K + ((size_t)bh * SK + (size_t)s * KEYS) * DH + lane * 8;
  const u16* vb = V + ((size_t)bh * SK + (size_t)s * KEYS) * DH + lane * 8;
  float m = -INFINITY, l = 0.f, acc[8] = {0};
  for (int t0 = wave * 64; t0 < KEYS; t0 += 256) {
    v4u kr[16], vr[16];
#pragma unroll
    for (int t = 0; t < 16; ++t) kr[t] = __builtin_nontemporal_load((const v4u*)(kb + (size_t)(t0 + 4 * t) * DH));
#pragma unroll
    for (int t = 0; t < 16; ++t) vr[t] = __builtin_nontemporal_load((const v4u*)(vb + (size_t)(t0 + 4 * t) * DH));
    float x[16];
#pragma unroll
    for (int t = 0; t < 16; ++t) { float d = 0; for (int k = 0; k < 4; ++k) d = dot2(kr[t][k], qv[k], d); x[t] = d; }
#define TR_STEP(HALF, CTRL, BIT) _Pragma("unroll") for (int h = 0; h < (HALF); ++h) { \
      const float lo_ = x[h], hi_ = x[h + (HALF)]; const float t1_ = lo_ + dpp<CTRL>(lo_), t2_ = hi_ + dpp<CTRL>(hi_); \
      x[h] = (c & (BIT)) ? t2_ : t1_; }
    TR_STEP(8, XOR8, 8) TR_STEP(4, XOR7, 4) TR_STEP(2, XOR2, 2) TR_STEP(1, XOR1, 1)
    float sc = rbf(rbf(x[0]) * 0.088388347648318440f) * 1.4426950408889634f;
    float pm = sc;
    for (int o = 32; o; o >>= 1) pm = fmaxf(pm, __shfl_xor(pm, o));
    float mn = fmaxf(m, pm);
    float alpha = __builtin_amdgcn_exp2f(m - mn), p = __builtin_amdgcn_exp2f(sc - mn);
    l = l * alpha + p; m = mn;
    for (int k = 0; k < 8; ++k) acc[k] *= alpha;
#pragma unroll
    for (int t = 0; t < 16; ++t) {
      float pt = __shfl(p, (lane & 48) + t);
      for (int k = 0; k < 4; ++k) { acc[2*k] += pt * lo(vr[t][k]); acc[2*k+1] += pt * hi(vr[t][k]); }
    }
  }
  for (int k = 0; k < 8; ++k) { acc[k] += __shfl_xor(acc[k], 16); acc[k] += __shfl_xor(acc[k], 32); }
  for (int o = 32; o; o >>= 1) l += __shfl_xor(l, o);
  if (r == 0) for (int k = 0; k < 8; ++k) mrg[wave][4 + 8 * c + k] = acc[k];
  if (lane == 0) { mrg[wave][0] = m; mrg[wave][1] = l; }
  __syncthreads();
  if (tid < 128) {
    float M = fmaxf(fmaxf(mrg[0][0], mrg[1][0]), fmaxf(mrg[2][0], mrg[3][0]));
    float L = 0, o = 0;
    for (int w = 0; w < 4; ++w) { float wt = __builtin_amdgcn_exp2f(mrg[w][0] - M); L += wt * mrg[w][1]; o += wt * mrg[w][4 + tid]; }
    float* pp = part + ((size_t)bh * NSPLIT + s) * 132;
    pp[4 + tid] = o; if (tid == 0) { pp[0] = M; pp[1] = L; }
  }
}

__global__ void __launch_bounds__(256) k_wo(const float* part, const u16* wo, float* wpart) {
  __shared__ float os[2][DH];
  int h = blockIdx.y, tid = threadIdx.x;
  { int b = tid >> 7, d = tid & 127; const float* pp = part + (size_t)(b * H + h) * NSPLIT * 132;
    float M = -INFINITY; for (int s = 0; s < NSPLIT; ++s) M = fmaxf(M, pp[s * 132]);
    float L = 0, o = 0; for (int s = 0; s < NSPLIT; ++s) { float wt = __builtin_amdgcn_exp2f(pp[s * 132] - M); L += wt * pp[s * 132 + 1]; o += wt * pp[s * 132 + 4 + d]; }
    os[b][d] = rbf(o / L); }
  __syncthreads();
  int cg = tid & 7, r0 = tid >> 3; int n0 = blockIdx.x * 64 + cg * 8;
  float a0[8] = {0}, a1[8] = {0};
  for (int r = r0; r < DH; r += 32) {
    v4u w = *(const v4u*)(wo + (size_t)(h * DH + r) * D + n0);
    float x0 = os[0][r], x1 = os[1][r];
    for (int j = 0; j < 4; ++j) { a0[2*j] += x0 * lo(w[j]); a0[2*j+1] += x0 * hi(w[j]); a1[2*j] += x1 * lo(w[j]); a1[2*j+1] += x1 * hi(w[j]); }
  }
  __shared__ float rr[2][32][64];
  for (int j = 0; j < 8; ++j) { rr[0][r0][cg*8+j] = a0[j]; rr[1][r0][cg*8+j] = a1[j]; }
  __syncthreads();
  if (tid < 128) { int b = tid >> 6, col = tid & 63; float s = 0; for (int r = 0; r < 32; ++r) s += rr[b][r][col]; wpart[(h * 2 + b) * D + blockIdx.x * 64 + col] = s; }
}

__global__ void __launch_bounds__(256) k_ffn1(const u16* x, const float* mod, const float* wpart, const u16* w1, const u16* w3, float* x1g, float* hh) {
  __shared__ float xs[2][D]; __shared__ float hs[2][D];
  int tid = threadIdx.x;
  for (int i = tid; i < 2 * D; i += 256) { int b = i / D, n = i % D; float s = 0; for (int h = 0; h < H; ++h) s += wpart[(h * 2 + b) * D + n];
    float v = rbf(bf2f(x[i]) + rbf(mod[b * 3072 + 1024 + n] * rbf(s))); xs[b][n] = v; if (blockIdx.x == 0) x1g[i] = v; }
  __syncthreads();
  compute_h(&xs[0][0], mod, 1536, 2048, hs, tid);
  int cg = tid & 3, r0 = tid >> 2; int n0 = blockIdx.x * 32 + cg * 8;
  float g0[8] = {0}, g1[8] = {0}, u0[8] = {0}, u1[8] = {0};
  for (int r = r0; r < D; r += 64) {
    v4u w = *(const v4u*)(w1 + (size_t)r * F + n0); v4u wu = *(const v4u*)(w3 + (size_t)r * F + n0);
    float x0 = hs[0][r], x1 = hs[1][r];
    for (int j = 0; j < 4; ++j) { g0[2*j] += x0 * lo(w[j]); g0[2*j+1] += x0 * hi(w[j]); g1[2*j] += x1 * lo(w[j]); g1[2*j+1] += x1 * hi(w[j]);
      u0[2*j] += x0 * lo(wu[j]); u0[2*j+1] += x0 * hi(wu[j]); u1[2*j] += x1 * lo(wu[j]); u1[2*j+1] += x1 * hi(wu[j]); }
  }
  __shared__ float rr[4][64][33];
  for (int j = 0; j < 8; ++j) { rr[0][r0][cg*8+j] = g0[j]; rr[1][r0][cg*8+j] = g1[j]; rr[2][r0][cg*8+j] = u0[j]; rr[3][r0][cg*8+j] = u1[j]; }
  __syncthreads();
  if (tid < 64) { int b = tid >> 5, col = tid & 31; float g = 0, u = 0; for (int r = 0; r < 64; ++r) { g += rr[b][r][col]; u += rr[2 + b][r][col]; }
    g = rbf(g); u = rbf(u); float sg = rbf(g / (1.f + __expf(-g))); hh[b * F + blockIdx.x * 32 + col] = rbf(sg * u); }
}

__global__ void __launch_bounds__(256) k_ffn2(const float* mod, const float* x1g, const float* hh, const u16* w2, u16* out) {
  __shared__ float hs[2][F];
  int tid = threadIdx.x;
  for (int i = tid; i < 2 * F; i += 256) hs[i / F][i % F] = hh[i];
  __syncthreads();
  int cg = tid & 1, r0 = tid >> 1; int n0 = blockIdx.x * 16 + cg * 8;
  float a0[8] = {0}, a1[8] = {0};
  for (int r = r0; r < F; r += 128) {
    v4u w = *(const v4u*)(w2 + (size_t)r * D + n0);
    float x0 = hs[0][r], x1 = hs[1][r];
    for (int j = 0; j < 4; ++j) { a0[2*j] += x0 * lo(w[j]); a0[2*j+1] += x0 * hi(w[j]); a1[2*j] += x1 * lo(w[j]); a1[2*j+1] += x1 * hi(w[j]); }
  }
  __shared__ float rr[2][128][17];
  for (int j = 0; j < 8; ++j) { rr[0][r0][cg*8+j] = a0[j]; rr[1][r0][cg*8+j] = a1[j]; }
  __syncthreads();
  if (tid < 32) { int b = tid >> 4, col = tid & 15; float s = 0; for (int r = 0; r < 128; ++r) s += rr[b][r][col];
    int n = blockIdx.x * 16 + col;
    float v = x1g[b * D + n] + rbf(mod[b * 3072 + 2560 + n] * rbf(s));
    __hip_bfloat16 hb = __float2bfloat16(v); out[b * D + n] = *(u16*)&hb; }
}

extern "C" void kernel_launch(void* const* d_in, const int* in_sizes, int n_in,
                              void* d_out, int out_size, void* d_ws, size_t ws_size,
                              hipStream_t stream) {
  const u16* x = (const u16*)d_in[0]; const u16* K = (const u16*)d_in[1]; const u16* V = (const u16*)d_in[2];
  const u16* wq = (const u16*)d_in[3]; const u16* wo = (const u16*)d_in[4];
  const u16* w1 = (const u16*)d_in[6]; const u16* w3 = (const u16*)d_in[7]; const u16* w2 = (const u16*)d_in[8];
  const u16* c = (const u16*)d_in[9]; const u16* wmod = (const u16*)d_in[10];
  char* ws = (char*)d_ws;
  float* mod = (float*)ws; u16* q = (u16*)(ws + 65536); float* part = (float*)(ws + 131072);
  float* wpart = (float*)(ws + (1 << 20)); float* x1g = (float*)(ws + (2 << 20)); float* hh = (float*)(ws + (3 << 20));
  k_mod<<<48, 256, 0, stream>>>(c, wmod, mod);
  k_q<<<64, 256, 0, stream>>>(x, mod, wq, q);
  k_attn<<<dim3(NSPLIT, 64), 256, 0, stream>>>(q, K, V, part);
  k_wo<<<dim3(8, 32), 256, 0, stream>>>(part, wo, wpart);
  k_ffn1<<<43, 256, 0, stream>>>(x, mod, wpart, w1, w3, x1g, hh);
  k_ffn2<<<32, 256, 0, stream>>>(mod, x1g, hh, w2, (u16*)d_out);
}
```

```cpp
#include <hip/hip_runtime.h>
#include <hip/hip_bf16.h>
#include <stdint.h>

typedef unsigned short u16;
typedef unsigned v4u __attribute__((ext_vector_type(4)));
typedef __bf16 bf16x2 __attribute__((ext_vector_type(2)));

constexpr int D = 512, H = 32, SK = 8192, DH = 128, F = 1376, NB = 2;
constexpr int NSPLIT = 8;

__device__ __forceinline__ float bf2f(u16 b) { return __uint_as_float((unsigned)b << 16); }
__device__ __forceinline__ float rbf(float f) { return __bfloat162float(__float2bfloat16(f)); }
__device__ __forceinline__ float lo(unsigned u) { return __uint_as_float(u << 16); }
__device__ __forceinline__ float hi(unsigned u) { return __uint_as_float(u & 0xffff0000u); }
__device__ __forceinline__ float dot2(unsigned k, unsigned q, float acc) {
  return __builtin_amdgcn_fdot2_f32_bf16(__builtin_bit_cast(bf16x2, k), __builtin_bit_cast(bf16x2, q), acc, false);
}

__device__ void gemv_cols(const float* xs, int K, const u16* W, int N, int n0, float* red  , float* out0, float* out1) {
}

__global__ void __launch_bounds__(256) k_mod(const u16* c, const u16* wmod, float* mod) {
  __shared__ float xs[2][D];
  __shared__ float red[2][32][8 + 1];
  int tid = threadIdx.x;
  for (int i = tid; i < 2 * D; i += 256) { float v = bf2f(c[i]); xs[i / D][i % D] = rbf(v / (1.f + __expf(-v))); }
  __syncthreads();
  int cg = tid & 7, r0 = tid >> 3; int n0 = blockIdx.x * 64 + cg * 8;
  float a0[8] = {0}, a1[8] = {0};
  for (int r = r0; r < D; r += 32) {
    v4u w = __builtin_nontemporal_load((const v4u*)(wmod + (size_t)r * 3072 + n0));
    float x0 = xs[0][r], x1 = xs[1][r];
    for (int j = 0; j < 4; ++j) { a0[2*j] += x0 * lo(w[j]); a0[2*j+1] += x0 * hi(w[j]); a1[2*j] += x1 * lo(w[j]); a1[2*j+1] += x1 * hi(w[j]); }
  }
  __shared__ float rr[2][32][64];
  for (int j = 0; j < 8; ++j) { rr[0][r0][cg*8+j] = a0[j]; rr[1][r0][cg*8+j] = a1[j]; }
  __syncthreads();
  if (tid < 128) { int b = tid >> 6, col = tid & 63; float s = 0; for (int r = 0; r < 32; ++r) s += rr[b][r][col]; mod[b * 3072 + blockIdx.x * 64 + col] = rbf(s); }
}

__device__ void ln_mod(const float* xrow  , const float* sh, const float* sc, float* hout, int tid, float* scratch) {
}

__device__ __forceinline__ void compute_h(const float* xin  , const float* mod, int offs, int offc, float (*hs)[D], int tid) {
  __shared__ float stat[2][2];
  int wave = tid >> 6, lane = tid & 63;
  if (wave < 2) {
    float v[8]; float s = 0;
    for (int j = 0; j < 8; ++j) { v[j] = xin[wave * D + lane + 64 * j]; s += v[j]; }
    for (int m = 32; m; m >>= 1) s += __shfl_xor(s, m);
    float mean = s / D; float q = 0;
    for (int j = 0; j < 8; ++j) { float d = v[j] - mean; q += d * d; }
    for (int m = 32; m; m >>= 1) q += __shfl_xor(q, m);
    float var = rbf(q / D); mean = rbf(mean);
    float den = rbf(sqrtf(rbf(var + 1e-5f)));
    for (int j = 0; j < 8; ++j) {
      int i = lane + 64 * j;
      float n = rbf(rbf(v[j] - mean) / den);
      float h = rbf(rbf(n * rbf(1.f + mod[wave * 3072 + offc + i])) + mod[wave * 3072 + offs + i]);
      hs[wave][i] = h;
    }
  }
  __syncthreads();
}

__global__ void __launch_bounds__(256) k_q(const u16* x, const float* mod, const u16* wq, u16* q) {
  __shared__ float xs[2][D]; __shared__ float hs[2][D];
  int tid = threadIdx.x;
  for (int i = tid; i < 2 * D; i += 256) xs[i / D][i % D] = bf2f(x[i]);
  __syncthreads();
  compute_h(&xs[0][0], mod, 0, 512, hs, tid);
  int cg = tid & 7, r0 = tid >> 3; int n0 = blockIdx.x * 64 + cg * 8;
  float a0[8] = {0}, a1[8] = {0};
  for (int r = r0; r < D; r += 32) {
    v4u w = __builtin_nontemporal_load((const v4u*)(wq + (size_t)r * 4096 + n0));
    float x0 = hs[0][r], x1 = hs[1][r];
    for (int j = 0; j < 4; ++j) { a0[2*j] += x0 * lo(w[j]); a0[2*j+1] += x0 * hi(w[j]); a1[2*j] += x1 * lo(w[j]); a1[2*j+1] += x1 * hi(w[j]); }
  }
  __shared__ float rr[2][32][64];
  for (int j = 0; j < 8; ++j) { rr[0][r0][cg*8+j] = a0[j]; rr[1][r0][cg*8+j] = a1[j]; }
  __syncthreads();
  if (tid < 128) { int b = tid >> 6, col = tid & 63; float s = 0; for (int r = 0; r < 32; ++r) s += rr[b][r][col];
    __hip_bfloat16 hb = __float2bfloat16(s); q[b * 4096 + blockIdx.x * 64 + col] = *(u16*)&hb; }
}

template <int CTRL> __device__ __forceinline__ float dpp(float x) {
  return __builtin_bit_cast(float, __builtin_amdgcn_mov_dpp(__builtin_bit_cast(int, x), CTRL, 0xf, 0xf, true));
}
constexpr int XOR1 = 0xB1, XOR2 = 0x4E, XOR7 = 0x141, XOR8 = 0x128;

__global__ void __launch_bounds__(256) k_attn(const u16* q, const u16* K, const u16* V, float* part  ) {
  __shared__ float mrg[4][132];
  int s = blockIdx.x, bh = blockIdx.y, tid = threadIdx.x, lane = tid & 63, wave = tid >> 6, r = lane >> 4, c = lane & 15;
  constexpr int KEYS = SK / NSPLIT;
  v4u qv = *(const v4u*)(q + bh * DH + 8 * c);
  const u16* kb = K + ((size_t)bh * SK + (size_t)s * KEYS) * DH + lane * 8;
  const u16* vb = V + ((size_t)bh * SK + (size_t)s * KEYS) * DH + lane * 8;
  float m = -INFINITY, l = 0.f, acc[8] = {0};
  for (int t0 = wave * 64; t0 < KEYS; t0 += 256) {
    v4u kr[16], vr[16];
#pragma unroll
    for (int t = 0; t < 16; ++t) kr[t] = __builtin_nontemporal_load((const v4u*)(kb + (size_t)(t0 + 4 * t) * DH));
#pragma unroll
    for (int t = 0; t < 16; ++t) vr[t] = __builtin_nontemporal_load((const v4u*)(vb + (size_t)(t0 + 4 * t) * DH));
    float x[16];
#pragma unroll
    for (int t = 0; t < 16; ++t) { float d = 0; for (int k = 0; k < 4; ++k) d = dot2(kr[t][k], qv[k], d); x[t] = d; }
#define TR_STEP(HALF, CTRL, BIT) _Pragma("unroll") for (int h = 0; h < (HALF); ++h) { \
      const float lo_ = x[h], hi_ = x[h + (HALF)]; const float t1_ = lo_ + dpp<CTRL>(lo_), t2_ = hi_ + dpp<CTRL>(hi_); \
      x[h] = (c & (BIT)) ? t2_ : t1_; }
    TR_STEP(8, XOR8, 8) TR_STEP(4, XOR7, 4) TR_STEP(2, XOR2, 2) TR_STEP(1, XOR1, 1)
    float sc = rbf(rbf(x[0]) * 0.088388347648318440f) * 1.4426950408889634f;
    float pm = sc;
    for (int o = 32; o; o >>= 1) pm = fmaxf(pm, __shfl_xor(pm, o));
    float mn = fmaxf(m, pm);
    float alpha = __builtin_amdgcn_exp2f(m - mn), p = __builtin_amdgcn_exp2f(sc - mn);
    l = l * alpha + p; m = mn;
    for (int k = 0; k < 8; ++k) acc[k] *= alpha;
#pragma unroll
    for (int t = 0; t < 16; ++t) {
      float pt = __shfl(p, (lane & 48) + t);
      for (int k = 0; k < 4; ++k) { acc[2*k] += pt * lo(vr[t][k]); acc[2*k+1] += pt * hi(vr[t][k]); }
    }
  }
  for (int k = 0; k < 8; ++k) { acc[k] += __shfl_xor(acc[k], 16); acc[k] += __shfl_xor(acc[k], 32); }
  for (int o = 32; o; o >>= 1) l += __shfl_xor(l, o);
  if (r == 0) for (int k = 0; k < 8; ++k) mrg[wave][4 + 8 * c + k] = acc[k];
  if (lane == 0) { mrg[wave][0] = m; mrg[wave][1] = l; }
  __syncthreads();
  if (tid < 128) {
    float M = fmaxf(fmaxf(mrg[0][0], mrg[1][0]), fmaxf(mrg[2][0], mrg[3][0]));
    float L = 0, o = 0;
    for (int w = 0; w < 4; ++w) { float wt = __builtin_amdgcn_exp2f(mrg[w][0] - M); L += wt * mrg[w][1]; o += wt * mrg[w][4 + tid]; }
    float* pp = part + ((size_t)bh * NSPLIT + s) * 132;
    pp[4 + tid] = o; if (tid == 0) { pp[0] = M; pp[1] = L; }
  }
}

__global__ void __launch_bounds__(256) k_wo(const float* part, const u16* wo, float* wpart) {
  __shared__ float os[2][DH];
  int h = blockIdx.y, tid = threadIdx.x;
  { int b = tid >> 7, d = tid & 127; const float* pp = part + (size_t)(b * H + h) * NSPLIT * 132;
    float M = -INFINITY; for (int s = 0; s < NSPLIT; ++s) M = fmaxf(M, pp[s * 132]);
    float L = 0, o = 0; for (int s = 0; s < NSPLIT; ++s) { float wt = __builtin_amdgcn_exp2f(pp[s * 132] - M); L += wt * pp[s * 132 + 1]; o += wt * pp[s * 132 + 4 + d]; }
    os[b][d] = rbf(o / L); }
  __syncthreads();
  int cg = tid & 7, r0 = tid >> 3; int n0 = blockIdx.x * 64 + cg * 8;
  float a0[8] = {0}, a1[8] = {0};
  for (int r = r0; r < DH; r += 32) {
    v4u w = __builtin_nontemporal_load((const v4u*)(wo + (size_t)(h * DH + r) * D + n0));
    float x0 = os[0][r], x1 = os[1][r];
    for (int j = 0; j < 4; ++j) { a0[2*j] += x0 * lo(w[j]); a0[2*j+1] += x0 * hi(w[j]); a1[2*j] += x1 * lo(w[j]); a1[2*j+1] += x1 * hi(w[j]); }
  }
  __shared__ float rr[2][32][64];
  for (int j = 0; j < 8; ++j) { rr[0][r0][cg*8+j] = a0[j]; rr[1][r0][cg*8+j] = a1[j]; }
  __syncthreads();
  if (tid < 128) { int b = tid >> 6, col = tid & 63; float s = 0; for (int r = 0; r < 32; ++r) s += rr[b][r][col]; wpart[(h * 2 + b) * D + blockIdx.x * 64 + col] = s; }
}

__global__ void __launch_bounds__(256) k_ffn1(const u16* x, const float* mod, const float* wpart, const u16* w1, const u16* w3, float* x1g, float* hh) {
  __shared__ float xs[2][D]; __shared__ float hs[2][D];
  int tid = threadIdx.x;
  for (int i = tid; i < 2 * D; i += 256) { int b = i / D, n = i % D; float s = 0; for (int h = 0; h < H; ++h) s += wpart[(h * 2 + b) * D + n];
    float v = rbf(bf2f(x[i]) + rbf(mod[b * 3072 + 1024 + n] * rbf(s))); xs[b][n] = v; if (blockIdx.x == 0) x1g[i] = v; }
  __syncthreads();
  compute_h(&xs[0][0], mod, 1536, 2048, hs, tid);
  int cg = tid & 3, r0 = tid >> 2; int n0 = blockIdx.x * 32 + cg * 8;
  float g0[8] = {0}, g1[8] = {0}, u0[8] = {0}, u1[8] = {0};
  for (int r = r0; r < D; r += 64) {
    v4u w = __builtin_nontemporal_load((const v4u*)(w1 + (size_t)r * F + n0)); v4u wu = __builtin_nontemporal_load((const v4u*)(w3 + (size_t)r * F + n0));
    float x0 = hs[0][r], x1 = hs[1][r];
    for (int j = 0; j < 4; ++j) { g0[2*j] += x0 * lo(w[j]); g0[2*j+1] += x0 * hi(w[j]); g1[2*j] += x1 * lo(w[j]); g1[2*j+1] += x1 * hi(w[j]);
      u0[2*j] += x0 * lo(wu[j]); u0[2*j+1] += x0 * hi(wu[j]); u1[2*j] += x1 * lo(wu[j]); u1[2*j+1] += x1 * hi(wu[j]); }
  }
  __shared__ float rr[4][64][33];
  for (int j = 0; j < 8; ++j) { rr[0][r0][cg*8+j] = g0[j]; rr[1][r0][cg*8+j] = g1[j]; rr[2][r0][cg*8+j] = u0[j]; rr[3][r0][cg*8+j] = u1[j]; }
  __syncthreads();
  if (tid < 64) { int b = tid >> 5, col = tid & 31; float g = 0, u = 0; for (int r = 0; r < 64; ++r) { g += rr[b][r][col]; u += rr[2 + b][r][col]; }
    g = rbf(g); u = rbf(u); float sg = rbf(g / (1.f + __expf(-g))); hh[b * F + blockIdx.x * 32 + col] = rbf(sg * u); }
}

__global__ void __launch_bounds__(256) k_ffn2(const float* mod, const float* x1g, const float* hh, const u16* w2, u16* out) {
  __shared__ float hs[2][F];
  int tid = threadIdx.x;
  for (int i = tid; i < 2 * F; i += 256) hs[i / F][i % F] = hh[i];
  __syncthreads();
  int cg = tid & 1, r0 = tid >> 1; int n0 = blockIdx.x * 16 + cg * 8;
  float a0[8] = {0}, a1[8] = {0};
  for (int r = r0; r < F; r += 128) {
    v4u w = __builtin_nontemporal_load((const v4u*)(w2 + (size_t)r * D + n0));
    float x0 = hs[0][r], x1 = hs[1][r];
    for (int j = 0; j < 4; ++j) { a0[2*j] += x0 * lo(w[j]); a0[2*j+1] += x0 * hi(w[j]); a1[2*j] += x1 * lo(w[j]); a1[2*j+1] += x1 * hi(w[j]); }
  }
  __shared__ float rr[2][128][17];
  for (int j = 0; j < 8; ++j) { rr[0][r0][cg*8+j] = a0[j]; rr[1][r0][cg*8+j] = a1[j]; }
  __syncthreads();
  if (tid < 32) { int b = tid >> 4, col = tid & 15; float s = 0; for (int r = 0; r < 128; ++r) s += rr[b][r][col];
    int n = blockIdx.x * 16 + col;
    float v = x1g[b * D + n] + rbf(mod[b * 3072 + 2560 + n] * rbf(s));
    __hip_bfloat16 hb = __float2bfloat16(v); out[b * D + n] = *(u16*)&hb; }
}

extern "C" void kernel_launch(void* const* d_in, const int* in_sizes, int n_in,
                              void* d_out, int out_size, void* d_ws, size_t ws_size,
                              hipStream_t stream) {
  const u16* x = (const u16*)d_in[0]; const u16* K = (const u16*)d_in[1]; const u16* V = (const u16*)d_in[2];
  const u16* wq = (const u16*)d_in[3]; const u16* wo = (const u16*)d_in[4];
  const u16* w1 = (const u16*)d_in[6]; const u16* w3 = (const u16*)d_in[7]; const u16* w2 = (const u16*)d_in[8];
  const u16* c = (const u16*)d_in[9]; const u16* wmod = (const u16*)d_in[10];
  char* ws = (char*)d_ws;
  float* mod = (float*)ws; u16* q = (u16*)(ws + 65536); float* part = (float*)(ws + 131072);
  float* wpart = (float*)(ws + (1 << 20)); float* x1g = (float*)(ws + (2 << 20)); float* hh = (float*)(ws + (3 << 20));
  k_mod<<<48, 256, 0, stream>>>(c, wmod, mod);
  k_q<<<64, 256, 0, stream>>>(x, mod, wq, q);
  k_attn<<<dim3(NSPLIT, 64), 256, 0, stream>>>(q, K, V, part);
  k_wo<<<dim3(8, 32), 256, 0, stream>>>(part, wo, wpart);
  k_ffn1<<<43, 256, 0, stream>>>(x, mod, wpart, w1, w3, x1g, hh);
  k_ffn2<<<32, 256, 0, stream>>>(mod, x1g, hh, w2, (u16*)d_out);
}
```

```cpp
#include <hip/hip_runtime.h>
#include <hip/hip_bf16.h>
#include <stdint.h>
#include <type_traits>

#define GAS __attribute__((address_space(1)))
#define LAS __attribute__((address_space(3)))
typedef unsigned short u16;
typedef unsigned long long u64;
typedef unsigned v4u __attribute__((ext_vector_type(4)));
typedef unsigned v2u __attribute__((ext_vector_type(2)));
typedef float v4f __attribute__((ext_vector_type(4)));
typedef float v2f __attribute__((ext_vector_type(2)));
typedef short v4s __attribute__((ext_vector_type(4)));
typedef __bf16 bf2 __attribute__((ext_vector_type(2)));
typedef __bf16 bf8 __attribute__((ext_vector_type(8)));
typedef __amdgpu_buffer_rsrc_t rsrc_t;

constexpr int NI = 52;
constexpr int WP = 6;
constexpr int WD = 16;
constexpr int WW = 12;
constexpr int WV = 6;
constexpr int WV2 = 12;
constexpr int ADELAY = 100;
constexpr int VDELAY = 80;
constexpr int TAU = 112;
constexpr int TAUD = 2;
constexpr int TEND = 3900;
constexpr int XPH = 14;

constexpr int D = 512, H = 32, SK = 8192, DH = 128, F = 1376;
constexpr int NTHR = 512, NPK = 136, NP = 2 * NPK;
constexpr int AUX_NT = 2, AUX_SC1 = 16;
constexpr unsigned SENT = 0xFFFFFFFFu;
constexpr unsigned SPIN_CAP = 1u << 18;
constexpr float LOG2E = 1.4426950408889634f;
constexpr int OOB = 0x7FFF0000;
constexpr int WS_EPOCH = 0, WS_FLAG = 2048, WS_FLAGQ = 4096, WS_ML = 6144, WS_MOD2 = 8192, WS_MODG = 16384, WS_QP = 32768, WS_REC = WS_QP + 262144,
              WS_OP = WS_REC + 256 * 512, OP_BYTES = 43 * 4096, WS_AP = WS_OP + OP_BYTES, WS_END = WS_AP + 4 * 32768;
constexpr int L_W = 0, L_W2 = 65536, L_MOD2 = 98304, L_X = L_MOD2 + 8192, L_Q = L_X + 2048, L_HD = L_Q + 256, L_PART = L_HD + 512,
              L_ML = L_PART + 2048, L_RED = L_ML + 64, L_STAT = L_RED + 4096, L_FLG = L_STAT + 128, L_V = L_FLG + 32, L_ZERO = L_V + 32768,
              L_END = L_ZERO + 1024;
constexpr int F_Q = L_FLG, F_QP = L_FLG + 4, F_V = L_FLG + 8, F_CNT = L_FLG + 12, F_ML = L_FLG + 16;

__device__ __forceinline__ float lo16(unsigned u) { return __uint_as_float(u << 16); }
__device__ __forceinline__ float hi16(unsigned u) { return __uint_as_float(u & 0xffff0000u); }
__device__ __forceinline__ unsigned pk(float a, float b) { v2f v = {a, b}; return __builtin_bit_cast(unsigned, __builtin_convertvector(v, bf2)); }
__device__ __forceinline__ float rbf(float a) { return hi16(pk(a, a)); }
__device__ __forceinline__ float dot2(unsigned a, unsigned b, float c) { return __builtin_amdgcn_fdot2_f32_bf16(__builtin_bit_cast(bf2, a), __builtin_bit_cast(bf2, b), c, false); }
__device__ __forceinline__ v4f mfma(v4u a, v4u b, v4f c) { return __builtin_amdgcn_mfma_f32_16x16x32_bf16(__builtin_bit_cast(bf8, a), __builtin_bit_cast(bf8, b), c, 0, 0, 0); }
template <int C> __device__ __forceinline__ float dpp(float x) { return __builtin_bit_cast(float, __builtin_amdgcn_mov_dpp(__builtin_bit_cast(int, x), C, 0xf, 0xf, true)); }
template <int C> __device__ __forceinline__ unsigned dppu(unsigned x) { return (unsigned)__builtin_amdgcn_mov_dpp((int)x, C, 0xf, 0xf, true); }
constexpr int XOR1 = 0xB1, XOR2 = 0x4E, XOR7 = 0x141, ROR8 = 0x128, ROR4 = 0x124, ROR2 = 0x122, ROR1 = 0x121, SHL2 = 0x102, NBC = 0x150;
__device__ __forceinline__ float half32(float a, float b) { auto s = __builtin_amdgcn_permlane32_swap(__float_as_uint(a), __float_as_uint(b), false, false); return __uint_as_float(s[0]) + __uint_as_float(s[1]); }
__device__ __forceinline__ float half16(float a, float b) { auto s = __builtin_amdgcn_permlane16_swap(__float_as_uint(a), __float_as_uint(b), false, false); return __uint_as_float(s[0]) + __uint_as_float(s[1]); }
__device__ __forceinline__ float wave_sum(float v) { v += dpp<ROR8>(v); v += dpp<ROR4>(v); v += dpp<ROR2>(v); v += dpp<ROR1>(v); v = half16(v, v); return half32(v, v); }
__device__ __forceinline__ float wave_max(float v) {
  v = fmaxf(v, dpp<ROR8>(v)); v = fmaxf(v, dpp<ROR4>(v)); v = fmaxf(v, dpp<ROR2>(v)); v = fmaxf(v, dpp<ROR1>(v));
  auto s = __builtin_amdgcn_permlane16_swap(__float_as_uint(v), __float_as_uint(v), false, false); v = fmaxf(__uint_as_float(s[0]), __uint_as_float(s[1]));
  auto t = __builtin_amdgcn_permlane32_swap(__float_as_uint(v), __float_as_uint(v), false, false); return fmaxf(__uint_as_float(t[0]), __uint_as_float(t[1]));
}
__device__ __forceinline__ float silu(float v) { return v * __builtin_amdgcn_rcpf(1.f + __builtin_amdgcn_exp2f(-v * LOG2E)); }
__device__ __forceinline__ unsigned umax(unsigned a, unsigned b) { return a > b ? a : b; }
__device__ __forceinline__ unsigned umax2(v2u a) { return umax(a.x, a.y); }
__device__ __forceinline__ unsigned umax4(v4u a) { return umax(umax(a.x, a.y), umax(a.z, a.w)); }
__device__ __forceinline__ unsigned sanu(unsigned u) { return u < SENT ? u : SENT - 1u; }
__device__ __forceinline__ unsigned san(float f) { return sanu(__float_as_uint(f)); }
__device__ __forceinline__ float sum8(v4f a, v4f b) { const v4f t = a + b; const v2f u = v2f{t.x, t.y} + v2f{t.z, t.w}; return u.x + u.y; }
__device__ __forceinline__ v2f pair_sum(v4f v, bool odd) {
  const float k0 = odd ? v.z : v.x, k1 = odd ? v.w : v.y, s0 = odd ? v.x : v.z, s1 = odd ? v.y : v.w;
  return v2f{k0 + dpp<XOR1>(s0), k1 + dpp<XOR1>(s1)};
}
__device__ __forceinline__ rsrc_t mk(const void* p, int bytes) { return __builtin_amdgcn_make_buffer_rsrc((void*)p, 0, bytes, 0x00020000); }
#define SRD(p, bytes) v4u{(unsigned)(size_t)(p), (unsigned)((size_t)(p) >> 32) & 0xFFFFu, (unsigned)(bytes), 0x00020000u}
#define LD128(r, vo, so, aux) __builtin_amdgcn_raw_buffer_load_b128(r, vo, so, aux)
#define LD64(r, vo, so, aux) __builtin_amdgcn_raw_buffer_load_b64(r, vo, so, aux)
#define LD32(r, vo, so, aux) __builtin_amdgcn_raw_buffer_load_b32(r, vo, so, aux)
#define LD16(r, vo, so, aux) __builtin_amdgcn_raw_buffer_load_b16(r, vo, so, aux)
#define ST128(v, r, vo, so, aux) __builtin_amdgcn_raw_buffer_store_b128(v, r, vo, so, aux)
#define ST64(v, r, vo, so, aux) __builtin_amdgcn_raw_buffer_store_b64(v, r, vo, so, aux)
#define ST32(v, r, vo, so, aux) __builtin_amdgcn_raw_buffer_store_b32(v, r, vo, so, aux)
#define DMA(r, l, vo, so) __builtin_amdgcn_raw_ptr_buffer_load_lds(r, (LAS void*)(L + (l)), 16, vo, so, 0, AUX_NT)
#define ADMA(op, srd, l, vo, so, pol) do { unsigned keep_; \
    asm volatile("s_mov_b32 %0, m0\n\ts_mov_b32 m0, %2\n\ts_nop 4\n\t" op " %1, %3, %4 offen " pol "lds\n\ts_mov_b32 m0, %0" \
                 : "=&s"(keep_) : "v"(vo), "s"((unsigned)(size_t)(L + (l))), "s"(srd), "s"(so) : "memory"); } while (0)
#define F2(v) __builtin_bit_cast(v2f, v)
#define F4(v) __builtin_bit_cast(v4f, v)
#define SB() __builtin_amdgcn_sched_barrier(0)
#define MEMBAR() asm volatile("" ::: "memory")
#define BAR() asm volatile("s_barrier" ::: "memory")
#define WGBAR() asm volatile("s_waitcnt lgkmcnt(0)\n\ts_barrier" ::: "memory")
#define LGKM0() asm volatile("s_waitcnt lgkmcnt(0)" ::: "memory")
#define VMW(n) asm volatile("s_waitcnt vmcnt(%0)" ::"n"(n) : "memory")
#define LDSW(off) (*(volatile LAS unsigned*)(L + (off)))
#define UNTIL(t) asm volatile("1:\n\ts_memrealtime vcc\n\ts_waitcnt lgkmcnt(0)\n\ts_sub_u32 vcc_lo, vcc_lo, %0\n\ts_cmp_lt_i32 vcc_lo, 0\n\t" \
                              "s_cbranch_scc0 2f\n\ts_sleep 1\n\ts_branch 1b\n2:" ::"s"(t) : "vcc", "scc")

constexpr int unit_of(int p) { return p == 7 ? 16 : p == 10 ? 15 : p == 13 ? 14 : p - (p > 7) - (p > 10) - (p > 13); }
constexpr bool staged(int u) { return u >= NPK && ((u - NPK) >> 3 == 0 || (u - NPK) >> 3 == 7 || (u - NPK) >> 3 == 13); }
constexpr int NG = NP - 24;
constexpr int item_of(int v) { int u = 0; for (int k = 0;; ++u) { if (staged(u)) continue; if (k++ == v) return u; } }
constexpr int issued_after(int u) {
  int done = 0;
  for (int k = 0; k <= u; ++k) done += !staged(k);
  const int a = NI + (done < WD ? done : WD), b = done + WD, m = a > b ? a : b;
  return m < NG ? m : NG;
}
template <int A, int B, class Fn> __device__ __forceinline__ void static_for(Fn&& f) {
  if constexpr (B - A == 1) f(std::integral_constant<int, A>{});
  else if constexpr (B - A > 1) { static_for<A, (A + B) / 2>(f); static_for<(A + B) / 2, B>(f); }
}
__device__ __forceinline__ v4u frag(LAS unsigned char* a, int hi_off) {
  const v4s l = __builtin_amdgcn_ds_read_tr16_b64_v4i16((LAS v4s*)a), h = __builtin_amdgcn_ds_read_tr16_b64_v4i16((LAS v4s*)(a + hi_off));
  const v2u lu = __builtin_bit_cast(v2u, l), hu = __builtin_bit_cast(v2u, h);
  return v4u{lu.x, lu.y, hu.x, hu.y};
}
__device__ __forceinline__ v4u diag_mask(int m) {
  const unsigned hm = (m & 1) ? 0xFFFF0000u : 0x0000FFFFu; const int q = m >> 1;
  return v4u{q == 0 ? hm : 0u, q == 1 ? hm : 0u, q == 2 ? hm : 0u, q == 3 ? hm : 0u};
}

__global__ void __launch_bounds__(NTHR, 2)
fused_decode(const u16* __restrict__ X, const u16* __restrict__ Kc, const u16* __restrict__ Vc, const u16* __restrict__ Wq, const u16* __restrict__ Wo,
             const u16* __restrict__ W1, const u16* __restrict__ W3, const u16* __restrict__ W2, const u16* __restrict__ Cc, const u16* __restrict__ Wm,
             u16* __restrict__ Out, unsigned char* __restrict__ ws) {
  __shared__ __attribute__((aligned(16))) unsigned char smem[L_END];
  LAS unsigned char* L = (LAS unsigned char*)smem;
  u64 pc0; unsigned code_len;
  asm volatile("s_getpc_b64 %0\n.Lfd_code_begin:\n\ts_mov_b32 %1, .Lfd_code_end-.Lfd_code_begin" : "=s"(pc0), "=s"(code_len));
  asm volatile("" ::"s"(X), "s"(Kc), "s"(Vc), "s"(Wq), "s"(Wo), "s"(W1), "s"(W3), "s"(W2), "s"(Cc), "s"(Wm), "s"(Out), "s"(ws));
  const int lane = threadIdx.x & 63, w = __builtin_amdgcn_readfirstlane(threadIdx.x >> 6), g = lane >> 4, c = lane & 15;
  const int id = blockIdx.x, g4 = id >> 2, sp = id & 3;
  const int h = g4 & 31, b = g4 >> 5, j = 4 * b + sp;
  const int rot = (g4 >> 1) & 3, rs = (sp + rot) & 3;
  const bool hasC = rs == 0, hasD = rs == 2 && g4 < 43, hasF = rs == 3 && g4 < 16, hasG = g4 >= 43 && rs != 0 && (rs != 3 || g4 >= 58);
  const int gu = rs == 3 ? g4 - 16 : 2 * (g4 - 43) + rs - 1;
  const bool hasA = hasG && gu < 16;
  const int kg = g4 >> 3, cg = g4 & 7;
  const unsigned nut = g4 < 43 ? __builtin_rotateright32(0x110E1110u, 8 * rot) : 0x10101010u;
  const int nu = (nut >> (8 * sp)) & 31, u0 = ((nut * 0x01010100u) >> (8 * sp)) & 63;
  const size_t chunk = ((size_t)g4 * SK + (size_t)u0 * 128) * DH;
  const rsrc_t rw = mk(ws, WS_END);
  v2u ep = {0u, 0u};

  const v4u e4 = {SENT, SENT, SENT, SENT};
  const int qw = w == 5 ? 0 : w - 4, nq = w == 5 ? 2 : 1;
  const bool doG = hasG && w != 4;
  auto issueG = [&]() {
    const v4u sm = SRD(Wm, D * 3072 * 2), sc = SRD(Cc, 2048);
    ADMA("buffer_load_dwordx4", sc, L_MOD2 + qw * 1024, (c & 1) * 1024 + (c >> 1) * 64 + g * 16, (qw >> 1) * 512, "");
    const int dvo = (lane >> 3) * 6144 + (lane & 7) * 16;
#pragma unroll 1
    for (int f = 0; f < 16 * nq; ++f) ADMA("buffer_load_dwordx4", sm, L_W + qw * 16384 + f * 1024, dvo, (128 * qw + 8 * f) * 6144 + gu * 128, "nt ");
  };
  if (w >= 4) {
    if (hasA || w == 4) __builtin_amdgcn_s_setprio(3);
    if (doG && hasA) issueG();
    if (w == 4) {
      ST128(e4, rw, lane * 16, WS_QP + (h * 8 + j) * 1024, AUX_SC1);
      ST64((v2u{SENT, SENT}), rw, lane == 0 ? 0 : OOB, WS_ML + id * 8, AUX_SC1);
      *(LAS v4u*)(L + L_FLG + (lane & 1) * 16) = v4u{0u, 0u, 0u, 0u};
    } else if (w == 7) {
      ST128(e4, rw, lane < 32 ? lane * 16 : OOB, WS_REC + id * 512, AUX_SC1);
      if (hasC) {
#pragma unroll
        for (int i = 0; i < 2; ++i) ST128(e4, rw, (g >> 1) * 32768 + (g & 1) * 2048 + c * 16, WS_AP + i * 65536 + kg * 4096 + cg * 256, AUX_SC1);
      }
      if (hasD) {
#pragma unroll
        for (int i = 0; i < 4; ++i) ST128(e4, rw, lane * 16, WS_OP + g4 * 4096 + i * 1024, AUX_SC1);
      }
      if (hasG && !hasA) ST128(e4, rw, lane < 16 ? lane * 16 : OOB, WS_MOD2 + (gu - 16) * 256, AUX_SC1);
    } else if (w == 6) {
      const v4u sp_ = SRD(pc0 & ~127ull, code_len);
      ADMA("buffer_load_dword", sp_, L_RED, lane < 10 ? lane * 128 : OOB, (id >> 3) * 1280, "");
    }
  }
  if (__builtin_expect(w < 4, 1)) {
    const rsrc_t rk = mk(Kc + chunk, nu * 32768), rv = mk(Vc + chunk, nu * 32768);
    const int vo = lane * 16 + w * 4096;
    v4u R[NP];
#define PIECE(u) LD128((u) < NPK ? rk : rv, vo + ((u) & 3) * 1024, unit_of((u) % NPK >> 3) * 32768 + ((u) & 4) * 4096, AUX_NT)
    const int np = 2 * nu - 8 - (nu > 14) - (nu > 16);
    const unsigned tau = nu == 16 ? TAU : nu == 17 ? TAU - TAUD : TAU + 2 * TAUD, xcc = __builtin_amdgcn_s_getreg(20 | (3 << 11)) & 7;
    unsigned due = (unsigned)__builtin_amdgcn_s_memrealtime() + TEND - (np - 1) * tau - ((xcc & 1) * 4 + (xcc >> 1)) * XPH;
    auto request = [&](auto V) {
      constexpr int it = item_of(V), un = unit_of(it % NPK >> 3);
      if constexpr ((it & 7) == 0) { UNTIL(due); due += un < 14 || un < nu ? tau : 0u; }
      R[it] = PIECE(it);
    };
    if (hasA) { BAR(); __builtin_amdgcn_s_sleep(ADELAY); }
    static_for<0, NI>([&](auto U) {
      constexpr int u = U;
      R[u] = PIECE(u);
      if constexpr (u == WP - 1) { if (!hasA) BAR(); }
      if constexpr (u >= WP) VMW(WP);
      SB();
    });
    while (LDSW(F_Q) == 0u) __builtin_amdgcn_s_sleep(1);
    MEMBAR();
    const v4u q = *(LAS v4u*)(L + L_Q + c * 16);
    float S[9], xs[16];
    static_for<0, NPK>([&](auto U) {
      constexpr int u = U, v = u ? issued_after(u - 1) : NI, t = u & 15, T = u >> 4;
      if constexpr (v < issued_after(u)) request(std::integral_constant<int, v>{});
      xs[t] = dot2(R[u].w, q.w, dot2(R[u].z, q.z, dot2(R[u].y, q.y, dot2(R[u].x, q.x, 0.f))));
      if constexpr (t == 15 || u == NPK - 1) {
#define TR_STEP(HALF, CT, BIT) _Pragma("unroll") for (int i = 0; i < (HALF); ++i) { \
        const float a_ = xs[i] + dpp<CT>(xs[i]), b_ = xs[i + (HALF)] + dpp<CT>(xs[i + (HALF)]); xs[i] = (c & (BIT)) ? b_ : a_; }
        if constexpr (t == 15) { TR_STEP(8, ROR8, 8) }
        else {
#pragma unroll
          for (int i = 0; i < 8; ++i) xs[i] += dpp<ROR8>(xs[i]);
        }
        TR_STEP(4, XOR7, 4) TR_STEP(2, XOR2, 2) TR_STEP(1, XOR1, 1)
        const float sc = rbf(rbf(xs[0]) * 0.088388347648318440f);
        constexpr int ul = unit_of(2 * T), uh = t == 15 ? unit_of(2 * T + 1) : 99;
        if constexpr (ul < 14 && uh < 14) S[T] = sc;
        else S[T] = (c < 8 ? ul : uh) < nu ? sc : -INFINITY;
      }
      if constexpr (u >= NI || (u & 3) == 3) SB();
    });
    const float m = wave_max(fmaxf(fmaxf(fmaxf(fmaxf(S[0], S[1]), fmaxf(S[2], S[3])), fmaxf(fmaxf(S[4], S[5]), fmaxf(S[6], S[7]))), S[8]));
    unsigned P[9]; float l = 0.f;
#pragma unroll
    for (int T = 0; T < 9; ++T) { const float p = __builtin_amdgcn_exp2f((S[T] - m) * LOG2E); l += p; P[T] = pk(p, p); }
    l = wave_sum(l);
    if (lane == 0) { *(LAS v2f*)(L + L_ML + w * 8) = v2f{m, l}; LGKM0(); LDSW(F_ML + w * 4) = 1u; }
    const v4u mask = diag_mask(c);
    v4f acc = {0.f, 0.f, 0.f, 0.f};
    while (LDSW(F_V) == 0u) __builtin_amdgcn_s_sleep(1);
    MEMBAR();
    const int a7 = nu > 16 ? L_W + vo : L_ZERO + lane * 16, a13 = nu > 14 ? L_W2 + vo : L_ZERO + lane * 16, s7 = nu > 16 ? 1024 : 0, s13 = nu > 14 ? 1024 : 0;
    int aP = lane < 32 ? L_PART + w * 512 + (8 * c + 4 * g) * 4 : L_HD + c * 16;
    asm volatile("" : "+v"(aP));
    SB();
    static_for<NPK, NP>([&](auto U) {
      constexpr int u = U, v = issued_after(u - 1);
      if constexpr (v < issued_after(u)) request(std::integral_constant<int, v>{});
      if constexpr (staged(u) && (u & 7) == 0) {
        constexpr int p = (u - NPK) >> 3;
#pragma unroll
        for (int i = 0; i < 8; ++i) { const int k = (i >> 2) * 16 + (i & 3); R[u + i] = *(LAS v4u*)(L + (p == 0 ? L_V + vo + k * 1024 : p == 7 ? a7 + k * s7 : a13 + k * s13)); }
      }
      const unsigned pb = dppu<NBC + ((u - NPK) & 15)>(P[(u - NPK) >> 4]);
      acc = mfma(v4u{pb & mask.x, pb & mask.y, pb & mask.z, pb & mask.w}, R[u], acc);
      SB();
    });
    *(LAS v4f*)(L + aP) = acc;
  } else {
    WGBAR();
    asm volatile("s_load_dwordx2 %0, %1, 0x0\n\ts_waitcnt lgkmcnt(0)" : "=&s"(ep) : "s"(ws));
    const unsigned tag = ep.x + 1u;
    auto after_qpart = [&]() { while (LDSW(F_QP) == 0u) __builtin_amdgcn_s_sleep(4); };
    auto stageV = [&](int f0, int f1) {
      const rsrc_t rv = mk(Vc + chunk, nu * 32768);
#pragma unroll 1
      for (int f = f0; f < f1; ++f) {
        const int k = f >> 5, r = f & 31;
        DMA(rv, (k == 0 ? L_V : k == 1 ? L_W2 : L_W) + r * 1024, lane * 16, (k ? 12 + 2 * k : 0) * 32768 + r * 1024);
        if (f < 32) VMW(WV); else VMW(WV2);
      }
    };
    auto firstV = [&]() {
      VMW(0);
      ST64((v2u{tag, ep.y}), rw, lane == 0 ? 0 : OOB, WS_FLAG + id * 8, AUX_SC1);
      *(LAS v4u*)(L + L_ZERO + lane * 16) = v4u{0u, 0u, 0u, 0u};
      __builtin_amdgcn_s_sleep(VDELAY);
      stageV(0, 32);
    };
    if (w == 7 && !hasA) firstV();
    if (doG) {
      if (!hasA) { after_qpart(); issueG(); }
      if (w == 5) VMW(32); else VMW(16);
      const v4u c0 = *(LAS v4u*)(L + L_MOD2 + qw * 1024 + lane * 16);
      v4u sv;
#pragma unroll
      for (int i = 0; i < 4; ++i) sv[i] = pk(silu(lo16(c0[i])), silu(hi16(c0[i])));
      LAS unsigned char* fa = L + L_W + qw * 16384 + (8 * g + (c >> 2)) * 128 + (c & 3) * 8;
      asm volatile("" : "+v"(sv));
      VMW(0);
      v4f d[4];
#pragma unroll
      for (int t = 0; t < 4; ++t) d[t] = v4f{0.f, 0.f, 0.f, 0.f};
#pragma unroll 1
      for (int r = 0; r < nq; ++r) {
        v4u wf[16];
#pragma unroll
        for (int i = 0; i < 16; ++i) wf[i] = frag(fa + r * 16384 + (i >> 2) * 4096 + (i & 3) * 32, 512);
#pragma unroll
        for (int i = 0; i < 4; ++i) {
          const bool on = (c >> 1) == ((qw + r) & 1) * 4 + i;
          const v4u A = {on ? sv.x : 0u, on ? sv.y : 0u, on ? sv.z : 0u, on ? sv.w : 0u};
#pragma unroll
          for (int t = 0; t < 4; ++t) d[t] = mfma(A, wf[4 * i + t], d[t]);
        }
      }
      *(LAS v2f*)(L + L_RED + 1024 + qw * 512 + lane * 8) = v2f{half16(half32(d[0].x + d[0].z, d[2].x + d[2].z), half32(d[1].x + d[1].z, d[3].x + d[3].z)),
                                                               half16(half32(d[0].y + d[0].w, d[2].y + d[2].w), half32(d[1].y + d[1].w, d[3].y + d[3].w))};
      LGKM0();
      unsigned old = 0;
      if (lane == 0) old = __hip_atomic_fetch_add((LAS unsigned*)(L + F_CNT), 1u, __ATOMIC_RELAXED, __HIP_MEMORY_SCOPE_WORKGROUP);
      if (__builtin_amdgcn_readfirstlane(old) == 2u) {
        MEMBAR();
        LAS v2f* pp = (LAS v2f*)(L + L_RED + 1024 + lane * 8);
        const v2f v = pp[0] + (pp[128] + pp[192]);
        if (hasA) { ST128((v4u{pk(v.x, v.y), tag, ep.y, ~tag}), rw, lane * 16, WS_MODG + gu * 1024, AUX_SC1); }
        else { ST32(sanu(pk(v.x, v.y)), rw, lane * 4, WS_MOD2 + (gu - 16) * 256, AUX_SC1); }
      }
      __builtin_amdgcn_s_setprio(0);
    }
    if (w == 4) {
      const rsrc_t rq = mk(Wq, D * 4096 * 2), rx = mk(X, 2048);
      const v4u x0 = LD128(rx, lane * 16, 0, 0), x1 = LD128(rx, lane * 16, 1024, 0);
      const unsigned xs0 = LD16(rx, lane * 2, 128 * j, 0), xs1 = LD16(rx, lane * 2, 1024 + 128 * j, 0);
      SB();
      v4u wq[16];
#pragma unroll
      for (int t = 0; t < 16; ++t) wq[t] = LD128(rq, g * 8192 + c * 16, (64 * j + 4 * t) * 8192 + h * 256, AUX_NT);
      SB();
      VMW(20);
      ST64((v2u{tag, ep.y}), rw, lane == 0 ? 0 : OOB, WS_FLAGQ + (h * 8 + j) * 8, AUX_SC1);
      float n0, n1;
      {
        float s0 = 0.f, s1 = 0.f;
#pragma unroll
        for (int i = 0; i < 4; ++i) { s0 += lo16(x0[i]) + hi16(x0[i]); s1 += lo16(x1[i]) + hi16(x1[i]); }
        const float m0 = wave_sum(s0) * (1.f / 512.f), m1 = wave_sum(s1) * (1.f / 512.f);
        float q0 = 0.f, q1 = 0.f;
#pragma unroll
        for (int i = 0; i < 4; ++i) { float t = lo16(x0[i]) - m0; q0 = fmaf(t, t, q0); t = hi16(x0[i]) - m0; q0 = fmaf(t, t, q0); t = lo16(x1[i]) - m1; q1 = fmaf(t, t, q1); t = hi16(x1[i]) - m1; q1 = fmaf(t, t, q1); }
        const float d0 = rbf(__builtin_amdgcn_sqrtf(rbf(rbf(wave_sum(q0) * (1.f / 512.f)) + 1e-5f))), d1 = rbf(__builtin_amdgcn_sqrtf(rbf(rbf(wave_sum(q1) * (1.f / 512.f)) + 1e-5f)));
        n0 = rbf(rbf(lo16(xs0) - rbf(m0)) / d0); n1 = rbf(rbf(lo16(xs1) - rbf(m1)) / d1);
      }
      const v4u mask = diag_mask(c & 7);
      unsigned n = 0, sa, ca;
      {
        struct G3 { v4u s, c; v2u m; };
        auto ld = [&]() { MEMBAR(); G3 r; r.s = LD128(rw, lane * 16, WS_MODG + j * 1024, AUX_SC1); r.c = LD128(rw, lane * 16, WS_MODG + (8 + j) * 1024, AUX_SC1);
          r.m = LD64(rw, (lane & 7) * 8, WS_FLAGQ + h * 64, AUX_SC1); SB(); return r; };
        auto ok = [&](const G3& r) { return !__any(((r.s.y ^ tag) | (r.s.z ^ ep.y) | (r.s.w ^ ~tag) | (r.c.y ^ tag) | (r.c.z ^ ep.y) | (r.c.w ^ ~tag) | (r.m.x ^ tag) | (r.m.y ^ ep.y)) != 0u); };
        for (;;) { const G3 p0 = ld(); if (ok(p0) || ++n > SPIN_CAP) { sa = p0.s.x; ca = p0.c.x; break; } }
      }
      {
        const float h0 = rbf(n0 * rbf(1.f + lo16(ca))) + lo16(sa), h1 = rbf(n1 * rbf(1.f + hi16(ca))) + hi16(sa);
        *(LAS unsigned*)(L + L_HD + (((lane & 3) * 2 + 0) * 16 + (lane >> 2)) * 4) = pk(h0, h0);
        *(LAS unsigned*)(L + L_HD + (((lane & 3) * 2 + 1) * 16 + (lane >> 2)) * 4) = pk(h1, h1);
      }
      v4u hd[4];
#pragma unroll
      for (int i = 0; i < 4; ++i) hd[i] = *(LAS v4u*)(L + L_HD + ((g * 2 + (c >> 3)) * 16 + 4 * i) * 4);
      v4f acc = {0.f, 0.f, 0.f, 0.f}, acc2 = {0.f, 0.f, 0.f, 0.f};
#pragma unroll
      for (int t = 0; t < 16; t += 2) {
        const unsigned hv = hd[t >> 2][t & 3], hw = hd[t >> 2][(t & 3) + 1];
        acc = mfma(v4u{hv & mask.x, hv & mask.y, hv & mask.z, hv & mask.w}, wq[t], acc);
        acc2 = mfma(v4u{hw & mask.x, hw & mask.y, hw & mask.z, hw & mask.w}, wq[t + 1], acc2);
      }
      acc += acc2;
      ST128((v4u{san(acc.x), san(acc.y), san(acc.z), san(acc.w)}), rw, (g >> 1) * 512 + (8 * c + 4 * (g & 1)) * 4, WS_QP + (h * 8 + j) * 1024, AUX_SC1);
      LDSW(F_QP) = 1u;
      v4f qs; n = 0;
      {
        struct Q4 { v4u p[4]; };
        auto ld = [&]() { MEMBAR(); Q4 r;
#pragma unroll
          for (int i = 0; i < 4; ++i) r.p[i] = LD128(rw, (lane >> 5) * 1024 + (lane & 31) * 16, WS_QP + (h * 8 + 2 * i) * 1024 + b * 512, AUX_SC1);
          SB(); return r; };
        auto ok = [&](const Q4& r) { return !__any(umax(umax(umax4(r.p[0]), umax4(r.p[1])), umax(umax4(r.p[2]), umax4(r.p[3]))) == SENT); };
        auto sum = [&](const Q4& r) { return (F4(r.p[0]) + F4(r.p[1])) + (F4(r.p[2]) + F4(r.p[3])); };
        for (;;) { const Q4 p0 = ld(); if (ok(p0) || ++n > SPIN_CAP) { qs = sum(p0); break; } }
      }
      {
        const float q0 = half32(qs.x, qs.x), q1 = half32(qs.y, qs.y), q2 = half32(qs.z, qs.z), q3 = half32(qs.w, qs.w);
        if (lane < 32) *(LAS v2u*)(L + L_Q + lane * 8) = v2u{pk(q0, q1), pk(q2, q3)};
      }
      LGKM0();
      LDSW(F_Q) = 1u;
      for (;;) { const v4u f = *(volatile LAS v4u*)(L + F_ML); if (f.x & f.y & f.z & f.w) break; __builtin_amdgcn_s_sleep(8); }
      MEMBAR();
      const v2f ml = *(LAS v2f*)(L + L_ML + (lane & 3) * 8);
      float Ms = fmaxf(ml.x, dpp<XOR1>(ml.x)); Ms = fmaxf(Ms, dpp<XOR2>(Ms));
      float Ls = __builtin_amdgcn_exp2f((ml.x - Ms) * LOG2E) * ml.y; Ls += dpp<XOR1>(Ls); Ls += dpp<XOR2>(Ls);
      ST64((v2u{san(Ms), san(Ls)}), rw, lane == 0 ? 0 : OOB, WS_ML + id * 8, AUX_SC1);
      v2u gl; n = 0;
      for (;;) {
        MEMBAR();
        gl = LD64(rw, (lane & 3) * 8, WS_ML + g4 * 32, AUX_SC1);
        if (!__any(umax2(gl) == SENT) || ++n > SPIN_CAP) break;
        __builtin_amdgcn_s_sleep(8);
      }
      float Mg = fmaxf(__uint_as_float(gl.x), dpp<XOR1>(__uint_as_float(gl.x))); Mg = fmaxf(Mg, dpp<XOR2>(Mg));
      float Lg = __builtin_amdgcn_exp2f((__uint_as_float(gl.x) - Mg) * LOG2E) * __uint_as_float(gl.y); Lg += dpp<XOR1>(Lg); Lg += dpp<XOR2>(Lg);
      if (lane < 4) *(LAS float*)(L + L_ML + 32 + lane * 4) = __builtin_amdgcn_exp2f((ml.x - Mg) * LOG2E) / Lg;
      __builtin_amdgcn_s_setprio(0);
      asm volatile(".Lfd_code_end:");
    } else if (w == 6) {
      __builtin_amdgcn_s_setprio(2);
      after_qpart();
      if (hasC) {
        const rsrc_t ro = mk(Wo, 4096 * D * 2);
#pragma unroll 1
        for (int f = 0; f < 64; ++f) { DMA(ro, L_W + f * 1024, (lane >> 3) * 1024 + (lane & 7) * 16, (512 * kg + 8 * f) * 1024 + cg * 128); VMW(WW); }
      } else if (hasD) {
        const rsrc_t r1 = mk(W1, D * F * 2), r3 = mk(W3, D * F * 2), r2 = mk(W2, F * D * 2);
#pragma unroll 1
        for (int f = 0; f < 32; ++f) {
          DMA(r1, L_W + f * 1024, (lane >> 2) * (F * 2) + (lane & 3) * 16, 16 * f * (F * 2) + g4 * 64);
          DMA(r3, L_W + 32768 + f * 1024, (lane >> 2) * (F * 2) + (lane & 3) * 16, 16 * f * (F * 2) + g4 * 64);
          DMA(r2, L_W2 + f * 1024, lane * 16, (32 * g4 + f) * 1024);
          VMW(WW);
        }
      }
      VMW(0);
      __builtin_amdgcn_s_setprio(0);
    } else if (w == 7) {
      if (hasA) firstV();
      after_qpart();
      stageV(32, 32 * (1 + (nu > 14) + (nu > 16)));
      asm volatile("s_waitcnt vmcnt(0) lgkmcnt(0)" ::: "memory");
      LDSW(F_V) = 1u;
      if (hasC || hasD || hasF) {
        __builtin_amdgcn_s_setprio(2);
        __builtin_amdgcn_s_sleep(64);
        const rsrc_t rx = mk(X, 2048);
        const v4u x0 = LD128(rx, lane * 16, 0, 0), x1 = LD128(rx, lane * 16, 1024, 0);
        *(LAS v4u*)(L + L_X + lane * 16) = x0; *(LAS v4u*)(L + L_X + 1024 + lane * 16) = x1;
        unsigned n = 0;
        for (;;) {
          MEMBAR();
          const v4u f0 = LD128(rw, lane * 16, WS_FLAG, AUX_SC1), f1 = LD128(rw, lane * 16, WS_FLAG + 1024, AUX_SC1);
          const unsigned bad = (f0.x ^ tag) | (f0.z ^ tag) | (f1.x ^ tag) | (f1.z ^ tag) | (f0.y ^ ep.y) | (f0.w ^ ep.y) | (f1.y ^ ep.y) | (f1.w ^ ep.y);
          if (!__any(bad != 0u) || ++n > SPIN_CAP) break;
          __builtin_amdgcn_s_sleep(16);
        }
        if (!hasC) {
          v4u m[8]; n = 0;
          for (;;) {
            MEMBAR();
            unsigned t = 0;
#pragma unroll
            for (int i = 0; i < 8; ++i) m[i] = LD128(rw, lane * 16, WS_MOD2 + i * 1024, AUX_SC1);
#pragma unroll
            for (int i = 0; i < 8; ++i) t = umax(t, umax4(m[i]));
            if (!__any(t == SENT) || ++n > SPIN_CAP) break;
            __builtin_amdgcn_s_sleep(16);
          }
#pragma unroll
          for (int i = 0; i < 8; ++i) *(LAS v4u*)(L + L_MOD2 + i * 1024 + lane * 16) = m[i];
        }
        __builtin_amdgcn_s_setprio(0);
      }
    }
  }
  int aM = L_PART + lane * 8, oM = lane * 8;
  asm volatile("" : "+v"(aM), "+v"(oM));
  WGBAR();
  if (w == 0) {
    const v4f f = *(LAS v4f*)(L + L_ML + 32);
    v2f o = {0.f, 0.f};
#pragma unroll
    for (int i = 0; i < 4; ++i) o += f[i] * *(LAS v2f*)(L + aM + i * 512);
    ST64((v2u{san(o.x), san(o.y)}), rw, oM, WS_REC + id * 512, AUX_SC1);
  }
  const int q4 = c >> 2, p4 = c & 3, kb = c >> 3, rb = (c >> 2) & 1;
  const v4f z = {0.f, 0.f, 0.f, 0.f};
  const int apb = WS_AP + (id & 3) * 32768;
  if (hasC) {
    v4u wf[8];
#pragma unroll
    for (int i = 0; i < 8; ++i) wf[i] = frag(L + L_W + (64 * w + 32 * (i & 1) + 8 * g + q4) * 128 + (i >> 1) * 32 + p4 * 8, 512);
    const int ro = (rb * 128 + (w >> 1) * 4 + (p4 & 1)) * 512 + (64 * (w & 1) + 32 * kb + 8 * g + 4 * (p4 >> 1)) * 4, so = WS_REC + kg * 8192;
    int aR = L_RED + (lane * 8 + w) * 4, aRr = L_RED + (w & 1) * 2048 + lane * 32, oA = (g & 1) * 2048 + (32 * (w & 1) + 16 * (g >> 1) + c) * 4;
    asm volatile("" : "+v"(aR), "+v"(aRr), "+v"(oA));
    v4u a0, a1; unsigned n = 0;
    for (;;) {
      MEMBAR();
      a0 = LD128(rw, ro, so, AUX_SC1); a1 = LD128(rw, ro + 1024, so, AUX_SC1);
      if (!__any(umax(umax4(a0), umax4(a1)) == SENT) || ++n > SPIN_CAP) break;
    }
    const v2f o = pair_sum(F4(a0) + F4(a1), c & 1);
    const unsigned po = pk(o.x, o.y);
    const v4u A = {dppu<0x00>(po), dppu<0x55>(po), dppu<0xAA>(po), dppu<0xFF>(po)};
    float v[4];
#pragma unroll
    for (int t = 0; t < 4; ++t) { const v4f d0 = mfma(A, wf[2 * t], z), d1 = mfma(A, wf[2 * t + 1], z); v[t] = g < 2 ? d0.x : d1.x; }
    *(LAS float*)(L + aR) = half32(v[0], v[1]);
    *(LAS float*)(L + aR + 2048) = half32(v[2], v[3]);
    WGBAR();
    ST32(san(sum8(*(LAS v4f*)(L + aRr), *(LAS v4f*)(L + aRr + 16))), rw, oA, WS_AP + (w >> 1) * 32768 + kg * 4096 + cg * 256, AUX_SC1);
  } else if (hasD) {
    v4u wf[8], w2f[4];
#pragma unroll
    for (int i = 0; i < 8; ++i) wf[i] = frag(L + L_W + (i >> 2) * 32768 + (64 * w + 32 * (i & 1) + 8 * g + q4) * 64 + ((i >> 1) & 1) * 32 + p4 * 8, 256);
#pragma unroll
    for (int t = 0; t < 4; ++t) w2f[t] = frag(L + L_W2 + (8 * g + q4) * 1024 + (64 * w + 16 * t) * 2 + p4 * 8, 4096);
    const int k0 = 64 * w + 32 * kb + 8 * g + 2 * p4;
    const v2u tg = *(LAS v2u*)(L + L_MOD2 + k0 * 4), ts = *(LAS v2u*)(L + L_MOD2 + (512 + k0) * 4), tc = *(LAS v2u*)(L + L_MOD2 + (1024 + k0) * 4);
    const unsigned tx = *(LAS unsigned*)(L + L_X + (rb * 512 + k0) * 2);
    float ga0 = rb ? hi16(tg.x) : lo16(tg.x), ga1 = rb ? hi16(tg.y) : lo16(tg.y), sb0 = rb ? hi16(ts.x) : lo16(ts.x), sb1 = rb ? hi16(ts.y) : lo16(ts.y);
    float cb0 = rbf(1.f + (rb ? hi16(tc.x) : lo16(tc.x))), cb1 = rbf(1.f + (rb ? hi16(tc.y) : lo16(tc.y))), x0 = lo16(tx), x1 = hi16(tx);
    int aS = L_STAT + rb * 64, aSw = (lane & 0x2B) == 0 ? L_STAT + rb * 64 + w * 4 + (lane & 16) * 2 : L_HD + lane * 4, aR = L_RED + (lane * 8 + w) * 4;
    int aRr = L_RED + ((g >> 1) * 32 + (c & 1) * 16 + (g & 1) * 8 + (c >> 1)) * 32, oP = (g >> 1) * 2048 + (64 * w + 16 * (g & 1) + c) * 4, o0 = (p4 & 1) * 4096 + rb * 2048 + (k0 & ~3) * 4;
    asm volatile("" : "+v"(ga0), "+v"(ga1), "+v"(sb0), "+v"(sb1), "+v"(cb0), "+v"(cb1), "+v"(x0), "+v"(x1), "+v"(aS), "+v"(aSw), "+v"(aR), "+v"(aRr), "+v"(oP), "+v"(o0));
    const v4u sW = SRD(ws, WS_END);
    v4u a0 = {}, a1 = {}, a2 = {}, a3 = {};
    unsigned n = 0, pend = 0xFu;
    for (;;) {
      unsigned so = apb;
#define PL(i) "s_bitcmp1_b32 %[m], " #i "\n\ts_cbranch_scc0 1f\n\tbuffer_load_dwordx4 %[a" #i "], %[o], %[r], %[s] offen sc1\n1:\n\ts_add_u32 %[s], %[s], 0x2000\n\t"
      asm volatile(PL(0) PL(1) PL(2) PL(3) "s_waitcnt vmcnt(0)"
                   : [a0] "+v"(a0), [a1] "+v"(a1), [a2] "+v"(a2), [a3] "+v"(a3), [s] "+s"(so) : [m] "s"(pend), [o] "v"(o0), [r] "s"(sW) : "scc", "memory");
      if (!__any(umax(umax(umax4(a0), umax4(a1)), umax(umax4(a2), umax4(a3))) == SENT) || ++n > SPIN_CAP) break;
#define PB(i) (__any(umax4(a##i) == SENT) ? 1u << i : 0u)
      pend = __builtin_amdgcn_readfirstlane(PB(0) | PB(1) | PB(2) | PB(3));
    }
    const v2f av = pair_sum((F4(a0) + F4(a2)) + (F4(a1) + F4(a3)), c & 1);
    const unsigned pa = pk(av.x, av.y), pg = pk(ga0 * lo16(pa), ga1 * hi16(pa)), px = pk(x0 + lo16(pg), x1 + hi16(pg));
    const float y0 = lo16(px), y1 = hi16(px);
    float s1 = y0 + y1, s2 = fmaf(y0, y0, y1 * y1);
    s1 += dpp<XOR1>(s1); s2 += dpp<XOR1>(s2); s1 += dpp<XOR2>(s1); s2 += dpp<XOR2>(s2); s1 += dpp<ROR8>(s1); s2 += dpp<ROR8>(s2);
    float ss = half16(s1, s2); ss = half32(ss, ss);
    *(LAS float*)(L + aSw) = ss;
    WGBAR();
    const v4f t0 = *(LAS v4f*)(L + aS), t1 = *(LAS v4f*)(L + aS + 16), t2 = *(LAS v4f*)(L + aS + 32), t3 = *(LAS v4f*)(L + aS + 48);
    const float mean = sum8(t0, t1) * (1.f / 512.f);
    const float var = fmaxf(fmaf(-mean, mean, sum8(t2, t3) * (1.f / 512.f)), 0.f);
    const float ri = __builtin_amdgcn_rcpf(rbf(__builtin_amdgcn_sqrtf(rbf(rbf(var) + 1e-5f)))), mr = rbf(mean);
    const unsigned pd = pk(y0 - mr, y1 - mr), pn = pk(lo16(pd) * ri, hi16(pd) * ri), pm = pk(lo16(pn) * cb0, hi16(pn) * cb1), ph = pk(lo16(pm) + sb0, hi16(pm) + sb1);
    const v4u A = {dppu<0x00>(ph), dppu<0x55>(ph), dppu<0xAA>(ph), dppu<0xFF>(ph)};
    float t4[4];
#pragma unroll
    for (int t = 0; t < 4; ++t) { const v4f d0 = mfma(A, wf[2 * t], z), d1 = mfma(A, wf[2 * t + 1], z); t4[t] = g < 2 ? d0.x : d1.x; }
    *(LAS float*)(L + aR) = half32(t4[0], t4[1]);
    *(LAS float*)(L + aR + 2048) = half32(t4[2], t4[3]);
    WGBAR();
    const v4f r0 = *(LAS v4f*)(L + aRr), r1 = *(LAS v4f*)(L + aRr + 16), r2 = *(LAS v4f*)(L + aRr + 2048), r3 = *(LAS v4f*)(L + aRr + 2064);
    SB();
    const float gt = rbf(sum8(r0, r1)), up = rbf(sum8(r2, r3));
    const float hv = rbf(silu(gt)) * up;
    const unsigned hp = pk(hv, dpp<SHL2>(hv));
#define BC2(i) (unsigned)__builtin_amdgcn_update_dpp(__builtin_amdgcn_mov_dpp((int)hp, NBC + 4 * (i), 0xf, 0x3, false), (int)hp, NBC + 4 * (i) + 1, 0xf, 0xc, false)
    const v4u A2 = {BC2(0), BC2(1), BC2(2), BC2(3)};
    const v4f d0 = mfma(A2, w2f[0], z), d1 = mfma(A2, w2f[1], z), d2 = mfma(A2, w2f[2], z), d3 = mfma(A2, w2f[3], z);
    ST32(san((g & 1) ? d1.x : d0.x), rw, oP, WS_OP + g4 * 4096, AUX_SC1);
    ST32(san((g & 1) ? d3.x : d2.x), rw, oP + 128, WS_OP + g4 * 4096, AUX_SC1);
  } else if (hasF) {
    float xo = 0.f, gb = 0.f;
    int oo = ((lane >> 5) * 512 + 32 * g4 + (lane & 31)) * 2, aRr = L_RED + lane * 32;
    if (w == 7) {
      const int col = 32 * g4 + (lane & 31), eb = lane >> 5;
      const unsigned tg = *(LAS unsigned*)(L + L_MOD2 + col * 4), tb = *(LAS unsigned*)(L + L_MOD2 + (1536 + col) * 4);
      const float ga = eb ? hi16(tg) : lo16(tg), x0 = lo16(*(LAS u16*)(L + L_X + (eb * 512 + col) * 2));
      gb = eb ? hi16(tb) : lo16(tb);
      unsigned a[8], n = 0;
      for (;;) {
        MEMBAR();
        unsigned t = 0;
#pragma unroll
        for (int i = 0; i < 8; ++i) a[i] = LD32(rw, (eb * 512 + col) * 4, apb + i * 4096, AUX_SC1);
#pragma unroll
        for (int i = 0; i < 8; ++i) t = umax(t, a[i]);
        if (!__any(t == SENT) || ++n > SPIN_CAP) break;
      }
#define AF(i) __uint_as_float(a[i])
      const float av = ((AF(0) + AF(4)) + (AF(2) + AF(6))) + ((AF(1) + AF(5)) + (AF(3) + AF(7)));
      xo = rbf(x0 + rbf(ga * rbf(av)));
      ST32(ep.x + 1u, rw, lane == 0 && g4 == 0 ? 0 : OOB, WS_EPOCH, AUX_SC1);
      asm volatile("" : "+v"(xo), "+v"(gb), "+v"(oo), "+v"(aRr));
    } else if (w < 6) {
      const rsrc_t rp = mk(ws + WS_OP, OP_BYTES);
      const int o0 = (8 * w + g) * 4096 + kb * 2048 + g4 * 128 + (c & 7) * 16;
      int aR = L_RED + ((kb * 32 + (c & 7) * 4 + g) * 8 + w) * 4;
      asm volatile("" : "+v"(aR));
      v4u a0, a1; unsigned n = 0;
      for (;;) {
        MEMBAR();
        a0 = LD128(rp, o0, 0, AUX_SC1); a1 = LD128(rp, o0, 16384, AUX_SC1);
        if (!__any(umax(umax4(a0), umax4(a1)) == SENT) || ++n > SPIN_CAP) break;
      }
      const v4f v = F4(a0) + F4(a1);
      *(LAS float*)(L + aR) = half16(half32(v.x, v.z), half32(v.y, v.w));
    }
    WGBAR();
    if (w == 7) {
      const v4f r0 = *(LAS v4f*)(L + aRr); const v2f r1 = *(LAS v2f*)(L + aRr + 16);
      const float t = rbf(((r0.x + r0.y) + (r0.z + r0.w)) + (r1.x + r1.y));
      const float y = xo + rbf(gb * t);
      __builtin_amdgcn_raw_buffer_store_b16((u16)(pk(y, y) & 0xFFFFu), mk(Out, 2048), oo, 0, 0);
    }
  }
}

extern "C" void kernel_launch(void* const* d_in, const int* in_sizes, int n_in, void* d_out, int out_size, void* d_ws, size_t ws_size, hipStream_t stream) {
  (void)in_sizes; (void)n_in; (void)out_size; (void)ws_size;
  hipLaunchKernelGGL(fused_decode, dim3(256), dim3(NTHR), 0, stream, (const u16*)d_in[0], (const u16*)d_in[1], (const u16*)d_in[2], (const u16*)d_in[3],
                     (const u16*)d_in[4], (const u16*)d_in[6], (const u16*)d_in[7], (const u16*)d_in[8], (const u16*)d_in[9], (const u16*)d_in[10], (u16*)d_out,
                     (unsigned char*)d_ws);
}
```
